```python
import torch

def _softmax(x, axis=-1):
    return torch.softmax(x, dim=axis)
def _gelu(x):
    return torch.nn.functional.gelu(x, approximate="tanh")
def _silu(x):
    return torch.nn.functional.silu(x)
def _sigmoid(x):
    return torch.sigmoid(x)
def _layernorm(x, axis=-1, eps=1e-5):
    m = x.mean(dim=axis, keepdim=True)
    v = x.var(dim=axis, keepdim=True, unbiased=False)
    return (x - m) / torch.sqrt(v + eps)
def _scatter_add(tgt, idx, src, axis=0):
    return tgt.clone().index_add_(axis, idx, src)
def _segment_sum(src, seg, n):
    out = torch.zeros((n,) + src.shape[1:], dtype=src.dtype)
    return out.index_add_(0, seg, src)
def _pad(x, pw):
    flat = tuple(v for pair in reversed(pw) for v in pair)
    return torch.nn.functional.pad(x, flat)

kernel_name = 'recurrent_scan__sliding_block_cached__dense_activation_squared_relu__decode'

def setup_inputs():

    _w = (lambda a: (lambda t: t.to(torch.bfloat16) if t.is_floating_point() else t)(torch.as_tensor(a)))
    def _A_setup():
        B, Sq, D = 2, 1, 512
        Hq, Hkv, Sk, Dh = 32, 32, 4096, 128
        assert Hq % Hkv == 0, (Hq, Hkv)
        return {
            "x": _w((lambda *s: torch.randn(*s, dtype=torch.bfloat16) * 0.05)(B, Sq, D)),
            "k_cache": _w((lambda *s: torch.randn(*s, dtype=torch.bfloat16) * 0.8)(B, Hkv, Sk, Dh)),
            "v_cache": _w((lambda *s: torch.randn(*s, dtype=torch.bfloat16) * 0.2)(B, Hkv, Sk, Dh)),
            "wq": _w((lambda *s: torch.randn(*s, dtype=torch.bfloat16) * 0.8)(D, Hq * Dh) * (D ** -0.5)),
            "wo": _w((lambda *s: torch.randn(*s, dtype=torch.bfloat16) * 0.2)(Hq * Dh, D) * ((Hq * Dh) ** -0.5)),
        }
    _A_in = _A_setup()

    def _B_setup():
        B, S, D, F = 2, 1, 512, 1376
        return {
            "x": _w((lambda *s: torch.randn(*s, dtype=torch.bfloat16) * 0.05)(B, S, D)),
            "w1": _w((lambda *s: torch.randn(*s, dtype=torch.bfloat16) * 0.1)(D, F)),
            "b1": _w((lambda *s: torch.randn(*s, dtype=torch.bfloat16) * 0.8)(F)),
            "w2": _w((lambda *s: torch.randn(*s, dtype=torch.bfloat16) * 0.1)(F, D)),
            "b2": _w((lambda *s: torch.randn(*s, dtype=torch.bfloat16) * 6.4)(D)),
        }
    _B_in = _B_setup()

    return {
        "A_x": _A_in["x"],
        "A_k_cache": _A_in["k_cache"],
        "A_v_cache": _A_in["v_cache"],
        "A_wq": _A_in["wq"],
        "A_wo": _A_in["wo"],
        "B_x": _B_in["x"],
        "B_w1": _B_in["w1"],
        "B_b1": _B_in["b1"],
        "B_w2": _B_in["w2"],
        "B_b2": _B_in["b2"],
    }

def reference(A_x, A_k_cache, A_v_cache, A_wq, A_wo, B_x, B_w1, B_b1, B_w2, B_b2):
    def _A_ref(x, k_cache, v_cache, wq, wo):
        B, Sq, D = x.shape
        Hq, Dh = 32, 128
        Hkv, Sk = k_cache.shape[1], k_cache.shape[2]
        W = min(4096, max(256, Sk // 4))
        rep = Hq // Hkv
        q = (lambda x, *d: x.permute(*d))((x @ wq).reshape(B, Sq, Hq, Dh), 0, 2, 1, 3)
        k = (lambda x, n, axis: torch.repeat_interleave(x, n, dim=axis))(k_cache[:, :, Sk - W:, :], rep, axis=1)
        v = (lambda x, n, axis: torch.repeat_interleave(x, n, dim=axis))(v_cache[:, :, Sk - W:, :], rep, axis=1)
        scores = torch.einsum("bhqd,bhkd->bhqk", q, k) * (Dh ** -0.5)
        p = _softmax(scores, axis=-1)
        o = torch.einsum("bhqk,bhkd->bhqd", p, v)
        return (lambda x, *d: x.permute(*d))(o, 0, 2, 1, 3).reshape(B, Sq, Hq * Dh) @ wo

    def _B_ref(x, w1, b1, w2, b2):
        h = torch.einsum("bsd,df->bsf", x, w1) + b1
        zero = torch.zeros_like(h)
        r = torch.where(h > zero, h, zero)
        h = r * r
        out = torch.einsum("bsf,fd->bsd", h, w2) + b2
        return out

    x = A_x
    xn = _layernorm(x, axis=-1)
    s = _A_ref(xn, A_k_cache, A_v_cache, A_wq, A_wo)
    g = _silu(_B_ref(xn, B_w1, B_b1, B_w2, B_b2))
    return x + s * g
```

```cpp
#include <hip/hip_runtime.h>
#include <hip/hip_bf16.h>
#include <stdint.h>

typedef unsigned u32;
typedef unsigned short u16;
typedef u32 u32x4 __attribute__((ext_vector_type(4)));
typedef u32 u32x2 __attribute__((ext_vector_type(2)));
typedef float f32x4 __attribute__((ext_vector_type(4)));
typedef float f32x2 __attribute__((ext_vector_type(2)));
typedef short s16x4 __attribute__((ext_vector_type(4)));
typedef __bf16 bf16x8 __attribute__((ext_vector_type(8)));
typedef __bf16 bf16x2 __attribute__((ext_vector_type(2)));
#define GAS __attribute__((address_space(1)))
#define LAS __attribute__((address_space(3)))

namespace {
constexpr int DM = 512, NH = 32, DH = 128, SK = 4096, WIN = 1024, FF = 1376;
constexpr int WS_EPOCH = 0;
constexpr int WS_H1Q = 4096;
constexpr int WS_H1F = WS_H1Q + 32 * 2 * 8 * 512;
constexpr int WS_HA = WS_H1F + 32 * 3 * 8 * 128;
constexpr int WS_HM = WS_HA + 32 * 8 * 512;
constexpr int WS_HB = WS_HM + 32 * 8 * 64;
constexpr int WS_END = WS_HB + 256 * 1024;
constexpr int L_WA = 0;
constexpr int L_WB = 16384;
constexpr int L_XN = 22528;
constexpr int L_Q = 23552;
constexpr int L_H = 23808;
constexpr int L_O = 24064;
constexpr int L_Z = 24576;
constexpr int L_ACC = 24832;
constexpr int L_ML = 26880;
constexpr int L_FLG = 26944;
constexpr int L_BYTES = 84 * 1024;
constexpr unsigned SPIN_MAX = 1u << 16;
constexpr int AUX_NT = 2, AUX_SC1 = 16;
constexpr int PACE_T0 = 500;
constexpr int PACE_DT = 150;
constexpr int PACE_DT2 = 120;
constexpr int PACE_FREE = 1;
constexpr int DEPTH = 10;
constexpr int QPOS = 10;
constexpr int QLOOK = 2650;
constexpr int QSTEP = 16;
constexpr int ASLEEP = 10, ASTEP = 8;
constexpr int BDELAY = 14, BSTEP = 6;

__device__ __forceinline__ float bf_lo(u32 x) { return __uint_as_float(x << 16); }
__device__ __forceinline__ float bf_hi(u32 x) { return __uint_as_float(x & 0xffff0000u); }
__device__ __forceinline__ float rbf(float f) { return (float)(__bf16)f; }
__device__ __forceinline__ u32 pk_bf(float lo, float hi) {
  bf16x2 v = {(__bf16)lo, (__bf16)hi};
  return __builtin_bit_cast(u32, v);
}
template <int CTRL> __device__ __forceinline__ float dpp(float x) {
  return __builtin_bit_cast(float, __builtin_amdgcn_mov_dpp(__builtin_bit_cast(int, x), CTRL, 0xf, 0xf, true));
}
template <int CTRL> __device__ __forceinline__ u32 dppu(u32 x) { return (u32)__builtin_amdgcn_mov_dpp((int)x, CTRL, 0xf, 0xf, true); }
constexpr int XOR1 = 0xB1, XOR2 = 0x4E, XOR7 = 0x141, XOR8 = 0x128;
__device__ __forceinline__ float x16_add(float x) {
  auto s = __builtin_amdgcn_permlane16_swap(__float_as_uint(x), __float_as_uint(x), false, false);
  return __uint_as_float(s[0]) + __uint_as_float(s[1]);
}
__device__ __forceinline__ float x32_add(float x) {
  auto s = __builtin_amdgcn_permlane32_swap(__float_as_uint(x), __float_as_uint(x), false, false);
  return __uint_as_float(s[0]) + __uint_as_float(s[1]);
}
__device__ __forceinline__ float x16_max(float x) {
  auto s = __builtin_amdgcn_permlane16_swap(__float_as_uint(x), __float_as_uint(x), false, false);
  return fmaxf(__uint_as_float(s[0]), __uint_as_float(s[1]));
}
__device__ __forceinline__ float x32_max(float x) {
  auto s = __builtin_amdgcn_permlane32_swap(__float_as_uint(x), __float_as_uint(x), false, false);
  return fmaxf(__uint_as_float(s[0]), __uint_as_float(s[1]));
}
__device__ __forceinline__ float wave_sum(float x) {
  x += dpp<XOR1>(x); x += dpp<XOR2>(x); x += dpp<XOR7>(x); x += dpp<XOR8>(x);
  return x32_add(x16_add(x));
}
__device__ __forceinline__ float wave_max(float x) {
  x = fmaxf(x, dpp<XOR1>(x)); x = fmaxf(x, dpp<XOR2>(x)); x = fmaxf(x, dpp<XOR7>(x)); x = fmaxf(x, dpp<XOR8>(x));
  return x32_max(x16_max(x));
}
__device__ __forceinline__ u32 now32() {
  unsigned long long t;
  asm volatile("s_memrealtime %0\n\ts_waitcnt lgkmcnt(0)" : "=s"(t)::"memory");
  return (u32)t;
}
__device__ __forceinline__ float dot2(u32 a, u32 b, float c) {
  return __builtin_amdgcn_fdot2_f32_bf16(__builtin_bit_cast(bf16x2, a), __builtin_bit_cast(bf16x2, b), c, false);
}
__device__ __forceinline__ f32x4 mfma32(u32x4 a, u32x4 b, f32x4 c) {
  return __builtin_amdgcn_mfma_f32_16x16x32_bf16(__builtin_bit_cast(bf16x8, a), __builtin_bit_cast(bf16x8, b), c, 0, 0, 0);
}
__device__ __forceinline__ f32x4 mfma16(u32x2 a, u32x2 b, f32x4 c) {
  return __builtin_amdgcn_mfma_f32_16x16x16bf16_1k(__builtin_bit_cast(s16x4, a), __builtin_bit_cast(s16x4, b), c, 0, 0, 0);
}
__device__ __forceinline__ u32x2 tr_read(LAS char* p) {
  return __builtin_bit_cast(u32x2, __builtin_amdgcn_ds_read_tr16_b64_v4i16((LAS s16x4*)p));
}
__device__ __forceinline__ u32x4 cat(u32x2 a, u32x2 b) { return u32x4{a.x, a.y, b.x, b.y}; }
struct Tag { u32 t, b0, b1, b2, b3; };
__device__ __forceinline__ Tag mk_tag(u32 t) { return Tag{t, t & 0xffu, (t >> 8) & 0xffu, (t >> 16) & 0xffu, t >> 24}; }
__device__ __forceinline__ u32x4 enc(f32x4 v, const Tag& t) {
  u32x4 r;
  r.x = ((__float_as_uint(v.x) + 0x80u) & 0xffffff00u) | t.b0;
  r.y = ((__float_as_uint(v.y) + 0x80u) & 0xffffff00u) | t.b1;
  r.z = ((__float_as_uint(v.z) + 0x80u) & 0xffffff00u) | t.b2;
  r.w = ((__float_as_uint(v.w) + 0x80u) & 0xffffff00u) | t.b3;
  return r;
}
__device__ __forceinline__ bool tag_ok(u32x4 g, u32 tag) {
  const u32 a = __builtin_amdgcn_perm(g.y, g.x, 0x0c0c0400u), b = __builtin_amdgcn_perm(g.w, g.z, 0x04000c0cu);
  return (a | b) == tag;
}
__device__ __forceinline__ f32x4 dec(u32x4 g) {
  f32x4 r = {__uint_as_float(g.x & 0xffffff00u), __uint_as_float(g.y & 0xffffff00u), __uint_as_float(g.z & 0xffffff00u), __uint_as_float(g.w & 0xffffff00u)};
  return r;
}
template <int A> __device__ __forceinline__ void look(int off, __amdgpu_buffer_rsrc_t rs) {
  asm volatile("buffer_load_dwordx4 v[%c0:%c1], %2, %3, 0 offen sc1" ::"n"(200 + A), "n"(203 + A), "v"(off), "s"(rs) : "memory");
}
template <int A, int LEFT> __device__ __forceinline__ u32x4 seen(void) {
  u32x4 r;
  asm volatile("s_waitcnt vmcnt(%c4)\n\tv_mov_b32 %0, v%c5\n\tv_mov_b32 %1, v%c6\n\tv_mov_b32 %2, v%c7\n\tv_mov_b32 %3, v%c8"
               : "=v"(r.x), "=v"(r.y), "=v"(r.z), "=v"(r.w) : "n"(LEFT), "n"(200 + A), "n"(201 + A), "n"(202 + A), "n"(203 + A) : "memory");
  return r;
}
#define DRAIN_VM() asm volatile("s_waitcnt vmcnt(0)" ::: "memory")
#define BAR() do { asm volatile("s_waitcnt lgkmcnt(0)" ::: "memory"); __builtin_amdgcn_s_barrier(); asm volatile("" ::: "memory"); } while (0)
#define PIN(x) asm volatile("" : "+v"(x))
#define FENCE() __builtin_amdgcn_sched_barrier(0)

#define GLDS(off, gptr, imm, ldsdst) do { u32 keep_; asm volatile("s_mov_b32 %0, m0\n\ts_mov_b32 m0, %3\n\ts_nop 0\n\tglobal_load_lds_dwordx4 %1, %2 offset:" #imm " nt\n\ts_mov_b32 m0, %0" \
      : "=&s"(keep_) : "v"(off), "s"(gptr), "s"(ldsdst) : "memory"); } while (0)

__global__ void __launch_bounds__(512, 2)
recurrent_scan__sliding_block_cached__dense_activation_squared_relu__decode_kernel(const u16* __restrict__ x, const u16* __restrict__ kc, const u16* __restrict__ vc, const u16* __restrict__ wq,
                    const u16* __restrict__ wo, const u16* __restrict__ w1, const u16* __restrict__ b1, const u16* __restrict__ w2,
                    const u16* __restrict__ b2, u16* __restrict__ out, char* __restrict__ ws) {
  __shared__ __attribute__((aligned(16))) char lds_[L_BYTES];
  LAS char* lds = (LAS char*)lds_;
  const int lane = threadIdx.x & 63, wave = __builtin_amdgcn_readfirstlane(threadIdx.x >> 6);
  const int wg = blockIdx.x, h = (wg & 7) * 4 + (wg >> 6), jm = (wg >> 3) & 7;
  const int g = lane >> 4, c = lane & 15;
  asm volatile("" ::"s"(x), "s"(kc), "s"(vc), "s"(wq), "s"(wo), "s"(w1), "s"(b1), "s"(w2), "s"(b2), "s"(out), "s"(ws));
  const __amdgpu_buffer_rsrc_t rws = __builtin_amdgcn_make_buffer_rsrc(ws, 0, WS_END, 0x00020000);

  if (wave >= 4) {
    const u32 tstart = now32();
    const int kw = wave - 4, bb = jm >> 2, sp = jm & 3;
    const size_t base = ((size_t)(bb * NH + h) * SK + (SK - WIN) + sp * 256 + kw * 64) * DH;
    const __amdgpu_buffer_rsrc_t rk = __builtin_amdgcn_make_buffer_rsrc((void*)(kc + base), 0, 64 * DH * 2, 0x00020000);
    const __amdgpu_buffer_rsrc_t rv = __builtin_amdgcn_make_buffer_rsrc((void*)(vc + base), 0, 64 * DH * 2, 0x00020000);
    const int rot = (wg * 7 + kw * 3) & 15;
    const u32 voff = (u32)lane * 16u;
    const int c0 = 5 * h + min(h, 12);
    u32 wo_off[2], w2_off[2];
    {
      const int r8 = lane >> 3;
#pragma unroll
      for (int k = 0; k < 2; ++k) {
        const int row = 32 * kw + 8 * k + r8;
        const int ch = (lane & 7) ^ ((((row >> 1) & 1) | (((row >> 3) & 1) << 1)) << 1);
        wo_off[k] = (u32)(((128 * h + row) * DM + 64 * jm + ch * 8) * 2); PIN(wo_off[k]);
        const int ins = min(kw + 4 * k, 5), row2 = 8 * ins + r8;
        const int ch2 = (lane & 7) ^ ((((row2 >> 1) & 1) | (((row2 >> 3) & 1) << 1)) << 1);
        w2_off[k] = (u32)((min(8 * c0 + row2, FF - 1) * DM + 64 * jm + ch2 * 8) * 2); PIN(w2_off[k]);
      }
    }
    const u32 ldsb = (u32)(size_t)lds;
    u32x4 kk[16], vv[16];
    FENCE();
#define THROTTLE(n) do { if ((n) >= DEPTH) asm volatile("s_waitcnt vmcnt(%0)" ::"n"(DEPTH - 1) : "memory"); \
      if ((n) >= PACE_FREE && PACE_DT > 0) { while (now32() - tstart < (u32)((PACE_T0 + ((n) - PACE_FREE) * PACE_DT) / 10)) {} } } while (0)
#define LDKV(dst, rs, i) asm volatile("buffer_load_dwordx4 %0, %1, %2, %3 offen nt" : "=&v"(dst) : "v"(voff), "s"(rs), "s"((((i) + rot) & 15) << 10) : "memory")
#pragma unroll
    for (int i = 0; i < 16; ++i) { THROTTLE(i); LDKV(kk[i], rk, i); }
    static_assert(QPOS >= 9 && QPOS <= 15, "NEXTV covers 7 blocks");
#pragma unroll
    for (int i = 0; i < QPOS; ++i) { THROTTLE(16 + i); LDKV(vv[i], rv, i); }
    asm volatile("s_waitcnt vmcnt(%0)" ::"n"(QPOS) : "memory");
    asm volatile("" : "+v"(kk[0]), "+v"(kk[1]), "+v"(kk[2]), "+v"(kk[3]), "+v"(kk[4]), "+v"(kk[5]), "+v"(kk[6]), "+v"(kk[7]));
    asm volatile("" : "+v"(kk[8]), "+v"(kk[9]), "+v"(kk[10]), "+v"(kk[11]), "+v"(kk[12]), "+v"(kk[13]), "+v"(kk[14]), "+v"(kk[15]));
    u32 msk[4];
#pragma unroll
    for (int d = 0; d < 4; ++d) { msk[d] = ((c >> 1) == d) ? ((c & 1) ? 0xffff0000u : 0x0000ffffu) : 0u; PIN(msk[d]); }
    BAR();
    const u32x4 q = *(LAS u32x4*)(lds + L_Q + c * 16);
    const u32 tagv = *(LAS u32*)(lds + L_ML + 32);
    float xs[16];
#define QK(i) { float d = dot2(kk[i][0], q[0], 0.f); d = dot2(kk[i][1], q[1], d); d = dot2(kk[i][2], q[2], d); xs[i] = dot2(kk[i][3], q[3], d); PIN(xs[i]); }
#define NEXTV(k) do { FENCE(); if (QPOS + (k) < 16) { THROTTLE(16 + QPOS + (k)); LDKV(vv[(QPOS + (k)) & 15], rv, QPOS + (k)); } FENCE(); } while (0)
    QK(0) QK(8) QK(1) QK(9) QK(2) QK(10) QK(3) QK(11) QK(4) QK(12) QK(5) QK(13) QK(6) QK(14) QK(7) QK(15)
    NEXTV(0);
#define TR_STEP(HALF, CTRL, BIT) _Pragma("unroll") for (int u = 0; u < (HALF); ++u) { \
      const float lo_ = xs[u], hi_ = xs[u + (HALF)]; const float t1_ = lo_ + dpp<CTRL>(lo_), t2_ = hi_ + dpp<CTRL>(hi_); \
      xs[u] = (c & (BIT)) ? t2_ : t1_; }
    TR_STEP(8, XOR8, 8) TR_STEP(4, XOR7, 4) TR_STEP(2, XOR2, 2) TR_STEP(1, XOR1, 1)
#undef TR_STEP
    float sc = rbf(rbf(xs[0]) * 0.08838834764831845f);
    PIN(sc);
    NEXTV(1);
    const float mw = wave_max(sc);
    if (lane == 0) *(LAS float*)(lds + L_ML + kw * 4) = mw;
    BAR();
    NEXTV(2);
    const f32x4 m4 = *(LAS f32x4*)(lds + L_ML);
    const float m = fmaxf(fmaxf(m4.x, m4.y), fmaxf(m4.z, m4.w));
    const float p = __builtin_amdgcn_exp2f((sc - m) * 1.4426950408889634f);
    u32 pp = pk_bf(p, p);
    PIN(pp);
    const Tag tag = mk_tag(tagv);
    const float lw = wave_sum(p);
    u32x4 mlg = enc(f32x4{m, lw, 0.f, 0.f}, tag);
    PIN(mlg);
    u32x4 af[16];
#define AF(i) { const u32 pb = dppu<0x150 + (i)>(pp); af[i] = u32x4{pb & msk[0], pb & msk[1], pb & msk[2], pb & msk[3]}; PIN(af[i]); }
    AF(0) AF(1) AF(2) AF(3)
    NEXTV(3);
    AF(4) AF(5) AF(6) AF(7)
    NEXTV(4);
    AF(8) AF(9) AF(10) AF(11)
    NEXTV(5);
    NEXTV(6);
    if (lane == 0) __builtin_amdgcn_raw_buffer_store_b128(mlg, rws, WS_HM + ((h * 8 + jm) * 4 + kw) * 16, 0, AUX_SC1);
    FENCE();
#define PACE2(n) do { if (PACE_DT2 > 0) { while (now32() - tstart < (u32)((PACE_T0 + (32 - PACE_FREE) * PACE_DT + (n) * PACE_DT2) / 10)) {} } } while (0)
    {
      const u32 o2 = wo_off[0] + 16384u, o3 = wo_off[1] + 16384u;
      PACE2(0); GLDS(wo_off[0], wo, 0, ldsb + L_WA + (32 * kw) * 128);
      PACE2(1); GLDS(wo_off[1], wo, 0, ldsb + L_WA + (32 * kw + 8) * 128);
      AF(12) AF(13)
      PACE2(2); GLDS(o2, wo, 0, ldsb + L_WA + (32 * kw + 16) * 128);
      PACE2(3); GLDS(o3, wo, 0, ldsb + L_WA + (32 * kw + 24) * 128);
      AF(14) AF(15)
      PACE2(4); GLDS(w2_off[0], w2, 0, ldsb + L_WB + kw * 1024);
      PACE2(5); GLDS(w2_off[1], w2, 0, ldsb + L_WB + min(kw + 4, 5) * 1024);
    }
    FENCE();
    int accw = L_ACC + kw * 512 + (2 * c + g) * 16, accr = L_ACC + (lane & 31) * 16, recoff = WS_HA + (h * 8 + jm) * 512 + lane * 16;
    PIN(accw); PIN(accr); PIN(recoff);
    f32x4 acc = {0.f, 0.f, 0.f, 0.f};
#define PV(i) { asm volatile("s_waitcnt vmcnt(%1)" : "+v"(vv[i]) : "n"(7 + 15 - (i)) : "memory"); acc = mfma32(af[i], vv[i], acc); FENCE(); }
    PV(0) PV(1) PV(2) PV(3) PV(4) PV(5) PV(6) PV(7) PV(8) PV(9) PV(10) PV(11) PV(12) PV(13) PV(14) PV(15)
#undef AF
#undef PV
    if (g < 2) *(LAS f32x4*)(lds + accw) = acc;
    BAR();
    if (kw == 0) {
      const f32x4 s0 = *(LAS f32x4*)(lds + accr), s1 = *(LAS f32x4*)(lds + accr + 512), s2 = *(LAS f32x4*)(lds + accr + 1024), s3 = *(LAS f32x4*)(lds + accr + 1536);
      const f32x4 s = (s0 + s1) + (s2 + s3);
      if (lane < 32) __builtin_amdgcn_raw_buffer_store_b128(enc(s, tag), rws, recoff, 0, AUX_SC1);
      asm volatile("s_waitcnt vmcnt(1)" ::: "memory");
    } else {
      asm volatile("s_waitcnt vmcnt(0)" ::: "memory");
    }
    __builtin_amdgcn_s_barrier();
    if (kw != 1) return;
    const int ob = wg & 1, ocg = wg >> 1;
    const u32 xo = x[ob * DM + 4 * ocg + (lane & 3)], b2o = b2[4 * ocg + (lane & 3)];
    int fin = WS_HB + wg * 1024 + ((lane & 31) * 2 + (lane >> 5)) * 16; PIN(fin);
    asm volatile("" ::"v"(xo), "v"(b2o));
    __builtin_amdgcn_s_barrier();
    __builtin_amdgcn_s_barrier();
    {
      u32x4 gb;
      __builtin_amdgcn_s_sleep(BDELAY);
      look<24>(fin, rws);
      __builtin_amdgcn_s_sleep(BSTEP); look<28>(fin, rws);
      for (unsigned it = 0;; ++it) {
        gb = seen<24, 1>();
        if (__all(tag_ok(gb, tag.t)) || it >= SPIN_MAX) break;
        look<24>(fin, rws);
        gb = seen<28, 1>();
        if (__all(tag_ok(gb, tag.t))) break;
        look<28>(fin, rws);
      }
      const f32x4 v = dec(gb);
      const bool o1 = lane & 1, o2 = lane & 2;
      const float a0 = v[0] + dpp<XOR1>(v[0]), a1 = v[1] + dpp<XOR1>(v[1]), a2 = v[2] + dpp<XOR1>(v[2]), a3 = v[3] + dpp<XOR1>(v[3]);
      const float c01 = o1 ? a1 : a0, c23 = o1 ? a3 : a2;
      const float d01 = c01 + dpp<XOR2>(c01), d23 = c23 + dpp<XOR2>(c23);
      float t = o2 ? d23 : d01;
      t += dpp<0x124>(t); t += dpp<XOR8>(t);
      t = x16_add(t);
      auto sw = __builtin_amdgcn_permlane32_swap(__float_as_uint(t), __float_as_uint(t), false, false);
      const float sa = rbf(__uint_as_float(sw[0]));
      const float f = rbf(rbf(__uint_as_float(sw[1])) + bf_lo(b2o));
      const float gt = rbf(f * __builtin_amdgcn_rcpf(1.f + __builtin_amdgcn_exp2f(f * -1.4426950408889634f)));
      const float r = bf_lo(xo) + rbf(sa * gt);
      if (lane < 4) out[ob * DM + 4 * ocg + lane] = __builtin_bit_cast(u16, (__bf16)r);
      if (wg == 0 && lane == 0) __hip_atomic_store((GAS u32*)(ws + WS_EPOCH), tag.t, __ATOMIC_RELAXED, __HIP_MEMORY_SCOPE_AGENT);
      DRAIN_VM();
    }
    return;
  }

  const int w = wave;
  __builtin_amdgcn_s_setprio(3);
  asm volatile("" ::: "v200", "v201", "v202", "v203", "v204", "v205", "v206", "v207", "v208", "v209", "v210", "v211", "v212", "v213", "v214", "v215", "v216", "v217", "v218", "v219", "v220", "v221", "v222", "v223", "v224", "v225", "v226", "v227", "v228", "v229", "v230", "v231", "v232", "v233", "v234", "v235", "v236", "v237", "v238", "v239");
  u32x4 xa, xb2;
  u32 xk, xs2, epoch;
  u32x2 b1v;
  const int c0 = 5 * h + min(h, 12), nch = h < 12 ? 6 : 5;
  const int f0 = 16 * w + 4 * (lane & 3);
  {
    const u32 o1 = (u32)lane * 32u, o2 = (u32)(lane >> 5) * 1024u, o3 = o2 + (u32)(jm * 128 + (lane & 31) * 4), oz = 0u;
    asm volatile("global_load_dwordx4 %0, %5, %9\n\tglobal_load_dwordx4 %1, %5, %9 offset:16\n\tglobal_load_ushort %2, %6, %9\n\t"
                 "global_load_dword %3, %7, %9\n\tglobal_load_dword %4, %8, %10 sc1"
                 : "=&v"(xa), "=&v"(xb2), "=&v"(xk), "=&v"(xs2), "=&v"(epoch)
                 : "v"(o1), "v"(o2), "v"(o3), "v"(oz), "s"(x), "s"(ws + WS_EPOCH)
                 : "memory");
  }
  const u32 ldsw = (u32)(size_t)lds;
  {
    const u32 o0 = (u32)(((64 * jm + 16 * w + g) * 4096 + h * 128) * 2);
#pragma unroll
    for (int k = 0; k < 4; ++k) {
      const int ch = c ^ ((g << 2) | k);
      const u32 o = o0 + (u32)(k * 4 * 8192 + ch * 16);
      GLDS(o, wq, 0, ldsw + L_WA + (16 * w + 4 * k) * 256);
    }
#pragma unroll
    for (int k = 0; k < 2; ++k) {
      const int ins = min(w + 4 * k, 5);
      const int e = ins * 64 + lane, row = e / 6, ch = min(c0 + e % 6, FF / 8 - 1);
      const u32 o = (u32)(((64 * jm + row) * FF + ch * 8) * 2);
      GLDS(o, w1, 0, ldsw + L_WB + ins * 1024);
    }
  }
  {
    const u32 a1 = (u32)min(8 * c0 + f0, FF - 4) * 2u;
    asm volatile("global_load_dwordx2 %0, %1, %2" : "=&v"(b1v) : "v"(a1), "s"(b1) : "memory");
  }
  FENCE();
  const u32 tstart = now32();
  FENCE();
  const int tq = c >> 2, tp = c & 3;
  int aq[2][2], af1[2], axb, axw, oq1, of1;
#pragma unroll
  for (int u = 0; u < 2; ++u) {
#pragma unroll
    for (int t = 0; t < 2; ++t) {
      aq[t][u] = L_WA + (8 * g + 4 * u + tq) * 256 + (((4 * w + 2 * t + (tp >> 1)) ^ ((tq << 2) | ((2 * g + u) & 3))) * 16) + (tp & 1) * 8;
      PIN(aq[t][u]);
    }
    af1[u] = L_WB + (8 * g + 4 * u + tq) * 96 + (16 * min(w, 2) + 4 * tp) * 2;
    PIN(af1[u]);
  }
  axb = L_XN + w * 256 + (c & 1) * 128 + g * 16; PIN(axb);
  axw = L_XN + w * 256 + (lane >> 5) * 128 + (lane & 31) * 4; PIN(axw);
  oq1 = WS_H1Q + ((h * 2 + (c & 1)) * 8 + jm) * 512 + (8 * w + g) * 16; PIN(oq1);
  of1 = WS_H1F + ((h * 3 + min(w, 2)) * 8 + jm) * 128 + (c & 1) * 64 + g * 16; PIN(of1);
  int qoff = WS_H1Q + ((h * 2 + (jm >> 2)) * 8 + (lane >> 3)) * 512 + (8 * w + (lane & 7)) * 16; PIN(qoff);
  int foff = WS_H1F + ((h * 3 + min(w, 2)) * 8 + (lane >> 3)) * 128 + (lane & 7) * 16; PIN(foff);
  const int fb = (lane >> 2) & 1;
  if (w == 0 && lane < 16) *(LAS u32x4*)(lds + L_Z + lane * 16) = u32x4{0u, 0u, 0u, 0u};
  FENCE();
  asm volatile("s_waitcnt vmcnt(7)" : "+v"(xa), "+v"(xb2), "+v"(xk), "+v"(xs2), "+v"(epoch)::"memory");
  const Tag tag = mk_tag(epoch + 1u);
  {
    const float k0 = bf_lo(xk);
    f32x2 s1 = {0.f, 0.f}, s2 = {0.f, 0.f};
#pragma unroll
    for (int k = 0; k < 8; ++k) {
      const u32 v = k < 4 ? xa[k] : xb2[k - 4];
      const f32x2 d = f32x2{bf_lo(v), bf_hi(v)} - f32x2{k0, k0};
      s1 += d; s2 += d * d;
    }
    float a = s1.x + s1.y, b = s2.x + s2.y;
    a += dpp<XOR1>(a); b += dpp<XOR1>(b); a += dpp<XOR2>(a); b += dpp<XOR2>(b);
    a += dpp<XOR7>(a); b += dpp<XOR7>(b); a += dpp<XOR8>(a); b += dpp<XOR8>(b);
    a = x16_add(a); b = x16_add(b);
    const float mu = a * (1.f / DM);
    const float var = fmaxf(fmaf(-mu, mu, b * (1.f / DM)), 0.f);
    const float mb = rbf(k0 + mu);
    const float sd = rbf(__builtin_amdgcn_sqrtf(rbf(rbf(var) + 1e-5f)));
    const float rc = __builtin_amdgcn_rcpf(sd);
    const u32 d2 = pk_bf(bf_lo(xs2) - mb, bf_hi(xs2) - mb);
    *(LAS u32*)(lds + axw) = pk_bf(bf_lo(d2) * rc, bf_hi(d2) * rc);
  }
  if (w == 0 && lane == 0) *(LAS u32*)(lds + L_ML + 32) = tag.t;
  asm volatile("s_waitcnt vmcnt(3)" ::: "memory");
  auto meet = [&](int k) {
    LAS u32* f = (LAS u32*)(lds + L_FLG + 16 * k);
    const u32 v = tag.t ^ (0x9e3779b9u + (u32)k);
    if (lane == 0) f[w] = v;
    for (unsigned it = 0; it < SPIN_MAX * 16; ++it) {
      asm volatile("" ::: "memory");
      const u32 r = ((volatile LAS u32*)f)[lane & 3];
      if (__all(r == v)) break;
    }
    asm volatile("" ::: "memory");
  };
  meet(0);
  u32x4 xb[2];
  {
    u32x4 aw[2][2];
#pragma unroll
    for (int i = 0; i < 2; ++i) {
      xb[i] = *(LAS u32x4*)(lds + axb + i * 64);
#pragma unroll
      for (int t = 0; t < 2; ++t) aw[t][i] = cat(tr_read(lds + aq[t][0] + i * 8192), tr_read(lds + aq[t][1] + i * 8192));
    }
    const f32x4 z = {0.f, 0.f, 0.f, 0.f};
    f32x4 q0 = mfma32(aw[0][0], xb[0], z), q1 = mfma32(aw[1][0], xb[0], z);
    q0 = mfma32(aw[0][1], xb[1], q0); q1 = mfma32(aw[1][1], xb[1], q1);
    f32x4 qs;
#pragma unroll
    for (int k = 0; k < 4; ++k) { const float t1 = dpp<0x44>(q1[k]); qs[k] = (c & 2) ? t1 : q0[k]; }
    if (c < 4) __builtin_amdgcn_raw_buffer_store_b128(enc(qs, tag), rws, oq1 + (c >> 1) * 64, 0, AUX_SC1);
  }
  asm volatile("s_waitcnt vmcnt(1)" : "+v"(b1v)::"memory");
  meet(1);
  {
    const u32x4 a10 = cat(tr_read(lds + af1[0]), tr_read(lds + af1[1])), a11 = cat(tr_read(lds + af1[0] + 3072), tr_read(lds + af1[1] + 3072));
    f32x4 f1 = mfma32(a10, xb[0], f32x4{0.f, 0.f, 0.f, 0.f});
    f1 = mfma32(a11, xb[1], f1);
    if (c < 2 && w < 3) __builtin_amdgcn_raw_buffer_store_b128(enc(f1, tag), rws, of1, 0, AUX_SC1);
  }
  while (now32() - tstart < (u32)(QLOOK / 10)) {}
  {
    u32x4 gq;
    look<16>(qoff, rws);
    __builtin_amdgcn_s_sleep(QSTEP);
    look<20>(qoff, rws);
    for (unsigned it = 0;; ++it) {
      gq = seen<16, 1>();
      if (__all(tag_ok(gq, tag.t)) || it >= SPIN_MAX) break;
      look<16>(qoff, rws);
      gq = seen<20, 1>();
      if (__all(tag_ok(gq, tag.t))) break;
      look<20>(qoff, rws);
    }
    f32x4 q4 = dec(gq);
#pragma unroll
    for (int k = 0; k < 4; ++k) q4[k] += dpp<XOR8>(q4[k]);
#pragma unroll
    for (int k = 0; k < 4; ++k) q4[k] = x16_add(q4[k]);
#pragma unroll
    for (int k = 0; k < 4; ++k) q4[k] = x32_add(q4[k]);
    if (lane < 8) *(LAS u32x2*)(lds + L_Q + (32 * w + 4 * lane) * 2) = u32x2{pk_bf(q4[0], q4[1]), pk_bf(q4[2], q4[3])};
    BAR();
    look<0>(foff, rws);
    __builtin_amdgcn_s_barrier();
  }
  const int swz2 = ((tq >> 1) & 1) << 1;
  int awo[2], aw2b, aoB, ahB, ahB2, aoW, raoff, rmoff, hboff;
#pragma unroll
  for (int u = 0; u < 2; ++u) {
    awo[u] = L_WA + (8 * g + 4 * u + tq) * 128 + (((2 * w + (tp >> 1)) ^ (swz2 | ((g & 1) << 2))) * 16) + (tp & 1) * 8;
    PIN(awo[u]);
  }
  { const int row = 32 + 4 * g + tq; aw2b = L_WB + row * 128 + (((2 * w + (tp >> 1)) ^ ((((row >> 1) & 1) | (((row >> 3) & 1) << 1)) << 1)) * 16) + (tp & 1) * 8; PIN(aw2b); }
  const bool oon = !(c & 1) && c < 4, hon = (c & 1) && c < 4;
  aoB = oon ? L_O + (c >> 1) * 256 + g * 16 : L_Z; PIN(aoB);
  ahB = hon ? L_H + (c >> 1) * 128 + g * 16 : L_Z; PIN(ahB);
  ahB2 = hon ? L_H + (c >> 1) * 128 + 64 + g * 8 : L_Z; PIN(ahB2);
  const int rs = lane & 3, hf = (lane >> 2) & 1, rb = (lane >> 3) & 1;
  aoW = L_O + rb * 256 + (32 * w + 8 * g + 4 * hf) * 2; PIN(aoW);
  rmoff = WS_HM + (h * 8 + rb * 4 + rs) * 64 + g * 16; PIN(rmoff);
  raoff = WS_HA + (h * 8 + rb * 4 + rs) * 512 + (8 * w + 2 * g + hf) * 16; PIN(raoff);
  hboff = WS_HB + (((jm * 16 + w * 4 + g) << 1) | ((c >> 1) & 1)) * 1024 + (h * 2 + (c & 1)) * 16; PIN(hboff);
  {
    u32x4 gf = seen<0, 0>();
    if (!__all(tag_ok(gf, tag.t))) {
      for (unsigned it = 0; it < SPIN_MAX; ++it) {
        look<0>(foff, rws);
        gf = seen<0, 0>();
        if (__all(tag_ok(gf, tag.t))) break;
      }
    }
    f32x4 h4 = dec(gf);
#pragma unroll
    for (int k = 0; k < 4; ++k) h4[k] += dpp<XOR8>(h4[k]);
#pragma unroll
    for (int k = 0; k < 4; ++k) h4[k] = x16_add(h4[k]);
#pragma unroll
    for (int k = 0; k < 4; ++k) h4[k] = x32_add(h4[k]);
    float hv[4];
    const float bv[4] = {bf_lo(b1v.x), bf_hi(b1v.x), bf_lo(b1v.y), bf_hi(b1v.y)};
#pragma unroll
    for (int k = 0; k < 4; ++k) {
      float t = rbf(rbf(h4[k]) + bv[k]);
      t = t > 0.f ? t : 0.f;
      hv[k] = (f0 + k < 8 * nch) ? t * t : 0.f;
    }
    if (lane < 8 && w < 3) *(LAS u32x2*)(lds + L_H + fb * 128 + f0 * 2) = u32x2{pk_bf(hv[0], hv[1]), pk_bf(hv[2], hv[3])};
  }
  BAR();
  auto weights = [&](const u32x4 gm) {
    const float mm = __uint_as_float(gm.x & 0xffffff00u);
    const float ll = x32_add(x16_add(__uint_as_float(gm.y & 0xffffff00u)));
    float M = fmaxf(mm, dpp<XOR1>(mm)); M = fmaxf(M, dpp<XOR2>(M));
    const float wt = __builtin_amdgcn_exp2f((mm - M) * 1.4426950408889634f);
    float L = wt * ll;
    L += dpp<XOR1>(L); L += dpp<XOR2>(L);
    return wt * __builtin_amdgcn_rcpf(L);
  };
  float cw;
  __builtin_amdgcn_s_sleep(ASLEEP);
  look<4>(rmoff, rws); look<8>(raoff, rws);
  __builtin_amdgcn_s_sleep(ASTEP);
  look<12>(raoff, rws);
  FENCE();
  __builtin_amdgcn_s_barrier();
  asm volatile("" ::: "memory");
  u32x4 wof[4], w2a, hb;
  u32x2 w2b, hb2;
#pragma unroll
  for (int i = 0; i < 4; ++i) wof[i] = cat(tr_read(lds + awo[0] + i * 4096), tr_read(lds + awo[1] + i * 4096));
  w2a = cat(tr_read(lds + awo[0] + (L_WB - L_WA)), tr_read(lds + awo[1] + (L_WB - L_WA)));
  w2b = tr_read(lds + aw2b);
  hb = *(LAS u32x4*)(lds + ahB);
  hb2 = *(LAS u32x2*)(lds + ahB2);
  FENCE();
  {
    u32x4 gm = seen<4, 2>();
    if (!__all(tag_ok(gm, tag.t))) {
      for (unsigned it = 0; it < SPIN_MAX; ++it) {
        look<4>(rmoff, rws);
        gm = seen<4, 0>();
        if (__all(tag_ok(gm, tag.t))) break;
      }
    }
    cw = weights(gm);
    PIN(cw);
  }
  {
    u32x4 ga;
    for (unsigned it = 0;; ++it) {
      ga = seen<8, 1>();
      if (__all(tag_ok(ga, tag.t)) || it >= SPIN_MAX) break;
      look<8>(raoff, rws);
      ga = seen<12, 1>();
      if (__all(tag_ok(ga, tag.t))) break;
      look<12>(raoff, rws);
    }
    const f32x4 a4 = dec(ga);
    float o[4];
#pragma unroll
    for (int k = 0; k < 4; ++k) o[k] = cw * a4[k];
#pragma unroll
    for (int k = 0; k < 4; ++k) o[k] += dpp<XOR1>(o[k]);
#pragma unroll
    for (int k = 0; k < 4; ++k) o[k] += dpp<XOR2>(o[k]);
    if (rs == 0) *(LAS u32x2*)(lds + aoW) = u32x2{pk_bf(o[0], o[1]), pk_bf(o[2], o[3])};
  }
  FENCE();
  f32x4 yacc = mfma32(w2a, hb, f32x4{0.f, 0.f, 0.f, 0.f});
  yacc = mfma16(w2b, hb2, yacc);
  FENCE();
  BAR();
  {
    u32x4 ob_[4];
#pragma unroll
    for (int i = 0; i < 4; ++i) ob_[i] = *(LAS u32x4*)(lds + aoB + i * 64);
    FENCE();
    f32x4 y2 = mfma32(wof[1], ob_[1], f32x4{0.f, 0.f, 0.f, 0.f});
    yacc = mfma32(wof[0], ob_[0], yacc);
    y2 = mfma32(wof[3], ob_[3], y2);
    yacc = mfma32(wof[2], ob_[2], yacc);
    yacc += y2;
    if (c < 4) __builtin_amdgcn_raw_buffer_store_b128(enc(yacc, tag), rws, hboff, 0, AUX_SC1);
  }
  __builtin_amdgcn_s_barrier();
  DRAIN_VM();
}
}

extern "C" void kernel_launch(void* const* d_in, const int* in_sizes, int n_in, void* d_out, int out_size, void* d_ws, size_t ws_size,
                              hipStream_t stream) {
  recurrent_scan__sliding_block_cached__dense_activation_squared_relu__decode_kernel<<<256, 512, 0, stream>>>((const u16*)d_in[0], (const u16*)d_in[1], (const u16*)d_in[2], (const u16*)d_in[3],
                                               (const u16*)d_in[4], (const u16*)d_in[6], (const u16*)d_in[7], (const u16*)d_in[8],
                                               (const u16*)d_in[9], (u16*)d_out, (char*)d_ws);
}
```
